```python
import torch

def _softmax(x, axis=-1):
    return torch.softmax(x, dim=axis)

def _gelu(x):
    return torch.nn.functional.gelu(x, approximate='tanh')

def _silu(x):
    return torch.nn.functional.silu(x)

def _sigmoid(x):
    return torch.sigmoid(x)

def _layernorm(x, axis=-1, eps=1e-05):
    m = x.mean(dim=axis, keepdim=True)
    v = x.var(dim=axis, keepdim=True, unbiased=False)
    return (x - m) / torch.sqrt(v + eps)

def _scatter_add(tgt, idx, src, axis=0):
    return tgt.clone().index_add_(axis, idx, src)

def _segment_sum(src, seg, n):
    out = torch.zeros((n,) + src.shape[1:], dtype=src.dtype)
    return out.index_add_(0, seg, src)

def _pad(x, pw):
    flat = tuple((v for pair in reversed(pw) for v in pair))
    return torch.nn.functional.pad(x, flat)
kernel_name = 'adaLN_cond__gqa_block_cached__geglu_ffn__decode'

def setup_inputs():

    _w = lambda a: (lambda t: t.to(torch.bfloat16) if t.is_floating_point() else t)(torch.as_tensor(a))

    def _A_setup():
        B, Sq, D = (1, 1, 512)
        Hq, Hkv, Sk, Dh = (32, 8, 65536, 128)
        assert Hq % Hkv == 0, (Hq, Hkv)
        return {'x': _w((lambda *s: torch.randn(*s, dtype=torch.bfloat16) * 0.05)(B, Sq, D)), 'k_cache': _w((lambda *s: torch.randn(*s, dtype=torch.bfloat16) * 3.2)(B, Hkv, Sk, Dh)), 'v_cache': _w((lambda *s: torch.randn(*s, dtype=torch.bfloat16) * 0.4)(B, Hkv, Sk, Dh)), 'wq': _w((lambda *s: torch.randn(*s, dtype=torch.bfloat16) * 3.2)(D, Hq * Dh) * D ** (-0.5)), 'wo': _w((lambda *s: torch.randn(*s, dtype=torch.bfloat16) * 0.4)(Hq * Dh, D) * (Hq * Dh) ** (-0.5))}
    _A_in = _A_setup()

    def _B_setup():
        B, S, D, F = (1, 1, 512, 1792)
        return {'x': _w((lambda *s: torch.randn(*s, dtype=torch.bfloat16) * 0.05)(B, S, D)), 'w_gate': _w((lambda *s: torch.randn(*s, dtype=torch.bfloat16) * 0.05)(D, F)), 'w_up': _w((lambda *s: torch.randn(*s, dtype=torch.bfloat16) * 0.05)(D, F)), 'w_down': _w((lambda *s: torch.randn(*s, dtype=torch.bfloat16) * 0.05)(F, D))}
    _B_in = _B_setup()
    return {'A_x': _A_in['x'], 'A_k_cache': _A_in['k_cache'], 'A_v_cache': _A_in['v_cache'], 'A_wq': _A_in['wq'], 'A_wo': _A_in['wo'], 'B_x': _B_in['x'], 'B_w_gate': _B_in['w_gate'], 'B_w_up': _B_in['w_up'], 'B_w_down': _B_in['w_down'], 'c': _w((lambda *s: torch.randn(*s, dtype=torch.bfloat16) * 0.2)(1, 512) * 8.0), 'w_mod': _w((lambda *s: torch.randn(*s, dtype=torch.bfloat16) * 0.2)(512, 6 * 512) * 512 ** (-0.5) * 8.0)}

def reference(A_x, A_k_cache, A_v_cache, A_wq, A_wo, B_x, B_w_gate, B_w_up, B_w_down, c, w_mod):

    def _A_ref(x, k_cache, v_cache, wq, wo):
        B, Sq, D = x.shape
        Hq, Dh = (32, 128)
        Hkv = k_cache.shape[1]
        rep = Hq // Hkv
        q = (lambda x, *d: x.permute(*d))((x @ wq).reshape(B, Sq, Hq, Dh), 0, 2, 1, 3)
        k = (lambda x, n, axis: torch.repeat_interleave(x, n, dim=axis))(k_cache, rep, axis=1)
        v = (lambda x, n, axis: torch.repeat_interleave(x, n, dim=axis))(v_cache, rep, axis=1)
        scores = torch.einsum('bhqd,bhkd->bhqk', q, k) * Dh ** (-0.5)
        p = _softmax(scores, axis=-1)
        o = torch.einsum('bhqk,bhkd->bhqd', p, v)
        return (lambda x, *d: x.permute(*d))(o, 0, 2, 1, 3).reshape(B, Sq, Hq * Dh) @ wo

    def _B_ref(x, w_gate, w_up, w_down):
        return _gelu(x @ w_gate) * (x @ w_up) @ w_down
    x = A_x
    mod = (_silu(c) @ w_mod).reshape(c.shape[0], 6, 1, -1)
    sa, ca, ga, sb, cb, gb = (mod[:, 0].clone(), mod[:, 1].clone(), mod[:, 2].clone(), mod[:, 3].clone(), mod[:, 4].clone(), mod[:, 5].clone())
    h = _layernorm(x, axis=-1) * (1 + ca) + sa
    x = x + ga * _A_ref(h, A_k_cache, A_v_cache, A_wq, A_wo)
    h = _layernorm(x, axis=-1) * (1 + cb) + sb
    x = x + gb * _B_ref(h, B_w_gate, B_w_up, B_w_down)
    return x
```

```cpp
#include <hip/hip_runtime.h>
#include <stdint.h>

#define GAS __attribute__((address_space(1)))
#define LAS __attribute__((address_space(3)))
typedef unsigned short u16;
typedef unsigned long long u64;
typedef unsigned v4u __attribute__((ext_vector_type(4)));
typedef unsigned v2u __attribute__((ext_vector_type(2)));
typedef float f32x4 __attribute__((ext_vector_type(4)));
typedef float f32x2 __attribute__((ext_vector_type(2)));
typedef short s16x4 __attribute__((ext_vector_type(4)));
typedef __bf16 bf16x2 __attribute__((ext_vector_type(2)));
typedef __bf16 bf16x8 __attribute__((ext_vector_type(8)));
typedef GAS u64 gu64;
typedef GAS unsigned gu32;
#define RLX_AGENT __ATOMIC_RELAXED, __HIP_MEMORY_SCOPE_AGENT
#define SB() __builtin_amdgcn_sched_barrier(0)
#define CFENCE() asm volatile("" ::: "memory")
#define PIN(x) asm volatile("" : "+v"(x))

constexpr int NWG = 256, NTHR = 512;
constexpr int NMOD = 48, NSPEC = 112, NFFN = 112, XREP = 16;
constexpr unsigned SPIN_CAP = 1u << 17;

constexpr int T_SPAN = 3760;
constexpr int LEAD = 4;
constexpr int GAP1 = 4, GAP2 = 8;
constexpr int DT = T_SPAN * 256 / (122 + GAP2), STAGGER = 7;
constexpr int N_TILE = 4, N_MODPOLL = 14, N_MODCHECK = 18, N_QPOLL = 36, N_QCHECK = 40;
constexpr int WV = 24;
constexpr int U_WLOAD = 8, U_WSTAGE = 11;
constexpr int K_MLLOAD = 3, K_MLUSE = 6;
constexpr int khi(int u) { return u < 0 ? N_QCHECK : (N_QCHECK + ((64 + WV - N_QCHECK) * (u + 1) / 16 + 1) / 2 * 2); }
constexpr int urow(int u) { return ((u & 7) << 1) | ((u >> 3) << 8); }

constexpr size_t WS_EPOCH = 0;
constexpr size_t WS_T0 = 256;
constexpr size_t WS_MOD = 1024;
constexpr size_t WS_Q = WS_MOD + 1536 * 8;
constexpr size_t WS_X2 = WS_Q + 2048 * 8;
constexpr size_t WS_ML = WS_X2 + XREP * 256 * 8;
constexpr size_t WS_PA = WS_ML + 32 * 32 * 2 * 8;
constexpr size_t WS_PB = WS_PA + 64 * 32 * 64 * 8;
constexpr size_t WS_PD = WS_PB + 64 * 64 * 8 * 8;

constexpr int L_RED0 = 0, L_RED2 = L_RED0 + 512, L_RED3 = L_RED2 + 256, L_RED4 = L_RED3 + 64, L_Q = L_RED4 + 64, L_M = L_Q + 256, L_L = L_M + 32,
              L_F = L_L + 32, L_LN = L_F + 4, L_O = L_LN + 16, L_ACC = L_O + 512, ACC_H = 132, ACC_W = 4 * ACC_H, L_W = L_ACC + 8 * ACC_W,
              L_WD = L_W + 8192, L_END = L_W + 16384;

__device__ __forceinline__ float asf(unsigned u) { return __builtin_bit_cast(float, u); }
__device__ __forceinline__ unsigned asu(float f) { return __builtin_bit_cast(unsigned, f); }
__device__ __forceinline__ unsigned pk(float lo, float hi) { bf16x2 v = {(__bf16)lo, (__bf16)hi}; return __builtin_bit_cast(unsigned, v); }
__device__ __forceinline__ float blo(unsigned u) { return asf(u << 16); }
__device__ __forceinline__ float bhi(unsigned u) { return asf(u & 0xffff0000u); }
__device__ __forceinline__ float rbf(float x) { return blo(pk(x, x)); }
__device__ __forceinline__ float rcp(float x) { return __builtin_amdgcn_rcpf(x); }
__device__ __forceinline__ float ex2(float x) { return __builtin_amdgcn_exp2f(x); }
template <int C> __device__ __forceinline__ unsigned dppu(unsigned x) { return __builtin_amdgcn_mov_dpp(x, C, 0xf, 0xf, true); }
template <int C> __device__ __forceinline__ float dpp(float x) { return asf(dppu<C>(asu(x))); }
constexpr int XOR1 = 0xB1, XOR2 = 0x4E, XOR7 = 0x141, XOR8 = 0x128, ROR4 = 0x124, SHL4 = 0x104, SHL8 = 0x108, SHL12 = 0x10C, BC0 = 0x150;
__device__ __forceinline__ float x16_sum(float x) { auto s = __builtin_amdgcn_permlane16_swap(asu(x), asu(x), false, false); return asf(s[0]) + asf(s[1]); }
__device__ __forceinline__ float x32_sum(float x) { auto s = __builtin_amdgcn_permlane32_swap(asu(x), asu(x), false, false); return asf(s[0]) + asf(s[1]); }
__device__ __forceinline__ float x16_max(float x) { auto s = __builtin_amdgcn_permlane16_swap(asu(x), asu(x), false, false); return fmaxf(asf(s[0]), asf(s[1])); }
__device__ __forceinline__ float x32_max(float x) { auto s = __builtin_amdgcn_permlane32_swap(asu(x), asu(x), false, false); return fmaxf(asf(s[0]), asf(s[1])); }
__device__ __forceinline__ float red8(float x) { x += dpp<XOR8>(x); return x32_sum(x16_sum(x)); }
__device__ __forceinline__ float wave_sum(float x) { x += dpp<XOR1>(x); x += dpp<XOR2>(x); x += dpp<XOR7>(x); x += dpp<XOR8>(x); return x32_sum(x16_sum(x)); }
__device__ __forceinline__ float wave_max(float x) {
  x = fmaxf(x, dpp<XOR1>(x)); x = fmaxf(x, dpp<XOR2>(x)); x = fmaxf(x, dpp<XOR7>(x)); x = fmaxf(x, dpp<XOR8>(x)); return x32_max(x16_max(x));
}
__device__ __forceinline__ float sum8(const LAS float* p) { const f32x4 r = *(const LAS f32x4*)p + *(const LAS f32x4*)(p + 4); return (r[0] + r[1]) + (r[2] + r[3]); }
__device__ __forceinline__ f32x4 mfma(v4u a, v4u b, f32x4 c) {
  return __builtin_amdgcn_mfma_f32_16x16x32_bf16(__builtin_bit_cast(bf16x8, a), __builtin_bit_cast(bf16x8, b), c, 0, 0, 0);
}
__device__ __forceinline__ __amdgpu_buffer_rsrc_t rsrc(const void* p, int bytes) { return __builtin_amdgcn_make_buffer_rsrc((void*)p, 0, bytes, 0x00020000); }
__device__ __forceinline__ v4u ldnt(__amdgpu_buffer_rsrc_t r, int vo, int so) { return __builtin_amdgcn_raw_buffer_load_b128(r, vo, so, 2); }
__device__ __forceinline__ v4u ldsh(__amdgpu_buffer_rsrc_t r, int vo, int so) { return __builtin_amdgcn_raw_buffer_load_b128(r, vo, so, 0); }
__device__ __forceinline__ u64 ldg(const unsigned char* p) { return __hip_atomic_load((gu64*)p, RLX_AGENT); }
__device__ __forceinline__ void stg(unsigned char* p, unsigned tag, unsigned v) { __hip_atomic_store((gu64*)p, ((u64)tag << 32) | v, RLX_AGENT); }
__device__ __forceinline__ bool tg(u64 g, unsigned tag) { return (unsigned)(g >> 32) == tag; }
__device__ __forceinline__ unsigned make_tag(unsigned e, int lane) {
  unsigned x = lane < 31 ? e : 0u;
  x += dppu<XOR1>(x); x += dppu<XOR2>(x); x += dppu<XOR7>(x); x += dppu<XOR8>(x);
  return (__builtin_amdgcn_readlane(x, 0) + __builtin_amdgcn_readlane(x, 16)) * 0x9E3779B3u + 1013904223u;
}
__device__ __forceinline__ void bar() { asm volatile("s_waitcnt lgkmcnt(0)\n\ts_barrier" ::: "memory"); }
__device__ __forceinline__ unsigned now() { return (unsigned)__builtin_amdgcn_s_memrealtime(); }
constexpr float LOG2E = 1.4426950408889634f;

__device__ __forceinline__ v4u wfrag(const unsigned* img, int lane, int s) {
  const LAS char* p = (const LAS char*)img + ((lane >> 4) * 16 + ((lane >> 2) & 3)) * 32 + (lane & 3) * 8 + s * 256;
  const v2u lo = __builtin_bit_cast(v2u, __builtin_amdgcn_ds_read_tr16_b64_v4i16((LAS s16x4*)p));
  const v2u hi = __builtin_bit_cast(v2u, __builtin_amdgcn_ds_read_tr16_b64_v4i16((LAS s16x4*)(p + 128)));
  return v4u{lo[0], lo[1], hi[0], hi[1]};
}
__device__ __forceinline__ void vfrag(float v, v4u& b0, v4u& b1) {
  const unsigned p = pk(v, dpp<XOR1>(v));
  b0 = v4u{dppu<BC0 + 0>(p), dppu<BC0 + 2>(p), dppu<BC0 + 4>(p), dppu<BC0 + 6>(p)};
  b1 = v4u{dppu<BC0 + 8>(p), dppu<BC0 + 10>(p), dppu<BC0 + 12>(p), dppu<BC0 + 14>(p)};
}
__device__ __forceinline__ void stage_tile(unsigned* img, const v4u (&r)[8], int lane) {
#pragma unroll
  for (int i = 0; i < 8; ++i) *(v4u*)(img + ((lane >> 1) & 3) * 512 + (8 * i + (lane >> 3)) * 8 + (lane & 1) * 4) = r[i];
}
__device__ __forceinline__ void stage_pairs(unsigned* dst, v4u r0, v4u r1) {
  v4u e0, e1;
#pragma unroll
  for (int k = 0; k < 2; ++k) {
    e0[2 * k] = __builtin_amdgcn_perm(r1[k], r0[k], 0x05040100u); e0[2 * k + 1] = __builtin_amdgcn_perm(r1[k], r0[k], 0x07060302u);
    e1[2 * k] = __builtin_amdgcn_perm(r1[k + 2], r0[k + 2], 0x05040100u); e1[2 * k + 1] = __builtin_amdgcn_perm(r1[k + 2], r0[k + 2], 0x07060302u);
  }
  *(v4u*)dst = e0; *(v4u*)(dst + 4) = e1;
}
__device__ __forceinline__ float dot16(unsigned p, v4u wa, v4u wb, float e0, float e1) {
  asm volatile("s_nop 1\n\t"
      "v_dot2c_f32_bf16_dpp %0, %2, %3 row_newbcast:0 row_mask:0xf bank_mask:0xf\n\t"
      "v_dot2c_f32_bf16_dpp %1, %2, %4 row_newbcast:2 row_mask:0xf bank_mask:0xf\n\t"
      "v_dot2c_f32_bf16_dpp %0, %2, %5 row_newbcast:4 row_mask:0xf bank_mask:0xf\n\t"
      "v_dot2c_f32_bf16_dpp %1, %2, %6 row_newbcast:6 row_mask:0xf bank_mask:0xf\n\t"
      "v_dot2c_f32_bf16_dpp %0, %2, %7 row_newbcast:8 row_mask:0xf bank_mask:0xf\n\t"
      "v_dot2c_f32_bf16_dpp %1, %2, %8 row_newbcast:10 row_mask:0xf bank_mask:0xf\n\t"
      "v_dot2c_f32_bf16_dpp %0, %2, %9 row_newbcast:12 row_mask:0xf bank_mask:0xf\n\t"
      "v_dot2c_f32_bf16_dpp %1, %2, %10 row_newbcast:14 row_mask:0xf bank_mask:0xf\n\t"
      "s_nop 2"
      : "+v"(e0), "+v"(e1) : "v"(p), "v"(wa[0]), "v"(wa[1]), "v"(wa[2]), "v"(wa[3]), "v"(wb[0]), "v"(wb[1]), "v"(wb[2]), "v"(wb[3]));
  return e0 + e1;
}

#define RD_L(r0, r1, p, off) "global_load_dwordx2 v[" #r0 ":" #r1 "], %[" #p "], off offset:" #off " sc1\n\t"
#define RD_A(r0, r1, p, off) "global_atomic_add_x2 v[" #r0 ":" #r1 "], %[" #p "], v[254:255], off offset:" #off " sc0\n\t"
#define POLL_HEAD "s_mov_b64 %[sv], exec\n\ts_mov_b32 %[n], 0x20000\n\tv_mov_b32 v254, 0\n\tv_mov_b32 v255, 0\n\t"
#define POLL_CMP(w, m, r) "s_waitcnt vmcnt(" #w ")\n\tv_cmp_ne_u32 %[" #m "], %[tag], v" #r "\n\t"
#define POLL_AGAIN(m) "s_wqm_b64 exec, %[" #m "]\n\t"
#define POLL_TAIL "s_mov_b64 exec, %[sv]\n\ts_sub_u32 %[n], %[n], 1\n\ts_cbranch_scc0 1b\n\ts_waitcnt vmcnt(0)\n\t2:\n\t"
#define POLL_CLOB "vcc", "scc", "memory", "v246", "v247", "v248", "v249", "v250", "v251", "v252", "v253", "v254", "v255"
__device__ __forceinline__ float poll_sum4(const unsigned char* p, unsigned tag) {
  float r; unsigned n; u64 sv, m0, m1, m2, m3;
  asm volatile(POLL_HEAD RD_L(246, 247, p, 0) RD_L(248, 249, p, 512) RD_L(250, 251, p, 1024) RD_L(252, 253, p, 1536)
      "1:\n\t" POLL_CMP(3, m0, 247) POLL_CMP(2, m1, 249) "s_mov_b64 vcc, %[m0]\n\t" POLL_CMP(1, m2, 251) "s_or_b64 vcc, vcc, %[m1]\n\t" POLL_CMP(0, m3, 253)
      "s_or_b64 vcc, vcc, %[m2]\n\ts_or_b64 vcc, vcc, %[m3]\n\ts_cbranch_scc0 2f\n\t"
      POLL_AGAIN(m0) RD_L(246, 247, p, 0) POLL_AGAIN(m1) RD_L(248, 249, p, 512) POLL_AGAIN(m2) RD_L(250, 251, p, 1024) POLL_AGAIN(m3) RD_L(252, 253, p, 1536) POLL_TAIL
      "v_add_f32 v246, v246, v248\n\tv_add_f32 v250, v250, v252\n\tv_add_f32 %[r], v246, v250\n\ts_nop 1"
      : [r] "=&v"(r), [n] "=&s"(n), [sv] "=&s"(sv), [m0] "=&s"(m0), [m1] "=&s"(m1), [m2] "=&s"(m2), [m3] "=&s"(m3) : [p] "v"(p), [tag] "v"(tag) : POLL_CLOB);
  return r;
}
__device__ __forceinline__ float poll_one(const unsigned char* p, unsigned tag) {
  float r; unsigned n; u64 sv, m0;
  asm volatile(POLL_HEAD RD_A(252, 253, p, 0)
      "1:\n\t" POLL_CMP(0, m0, 253) "s_cmp_lg_u64 %[m0], 0\n\ts_cbranch_scc0 2f\n\t"
      POLL_AGAIN(m0) RD_A(252, 253, p, 0) POLL_TAIL
      "v_mov_b32 %[r], v252\n\ts_nop 1"
      : [r] "=&v"(r), [n] "=&s"(n), [sv] "=&s"(sv), [m0] "=&s"(m0) : [p] "v"(p), [tag] "v"(tag) : POLL_CLOB);
  return r;
}
__device__ __forceinline__ float poll_sum2(const unsigned char* p, unsigned tag) {
  float r; unsigned n; u64 sv, m0, m1;
  asm volatile(POLL_HEAD RD_A(250, 251, p, 0) RD_A(252, 253, p, 512)
      "1:\n\t" POLL_CMP(1, m0, 251) POLL_CMP(0, m1, 253) "s_or_b64 vcc, %[m0], %[m1]\n\ts_cbranch_scc0 2f\n\t"
      POLL_AGAIN(m0) RD_A(250, 251, p, 0) POLL_AGAIN(m1) RD_A(252, 253, p, 512) POLL_TAIL
      "v_add_f32 %[r], v250, v252\n\ts_nop 1"
      : [r] "=&v"(r), [n] "=&s"(n), [sv] "=&s"(sv), [m0] "=&s"(m0), [m1] "=&s"(m1) : [p] "v"(p), [tag] "v"(tag) : POLL_CLOB);
  return r;
}
__device__ __forceinline__ unsigned poll_two(const unsigned char* p, const unsigned char* q, unsigned tag, unsigned& r1) {
  unsigned r0, n; u64 sv, m0, m1;
  asm volatile(POLL_HEAD RD_L(250, 251, p, 0) RD_L(252, 253, q, 0)
      "1:\n\t" POLL_CMP(1, m0, 251) POLL_CMP(0, m1, 253) "s_or_b64 vcc, %[m0], %[m1]\n\ts_cbranch_scc0 2f\n\t"
      POLL_AGAIN(m0) RD_L(250, 251, p, 0) POLL_AGAIN(m1) RD_L(252, 253, q, 0) POLL_TAIL
      "v_mov_b32 %[r0], v250\n\tv_mov_b32 %[r1], v252\n\ts_nop 1"
      : [r0] "=&v"(r0), [r1] "=&v"(r1), [n] "=&s"(n), [sv] "=&s"(sv), [m0] "=&s"(m0), [m1] "=&s"(m1) : [p] "v"(p), [q] "v"(q), [tag] "v"(tag) : POLL_CLOB);
  return r0;
}

struct Args {
  const u16 *K, *wq, *wmod;
  unsigned char* ws;
  const u16 *x, *c, *V, *wo, *wg, *wu, *wd;
  u16* out;
};

__global__ void __launch_bounds__(NTHR, 2) fused_block(Args a) {
  __shared__ __attribute__((aligned(16))) unsigned lds[L_END];
  float* ldf = (float*)lds;
  const u16 *pK = a.K, *pq = a.wq, *pm = a.wmod;
  const unsigned t_start = now();
  SB();
  const int t = threadIdx.x, lane = t & 63, w = __builtin_amdgcn_readfirstlane(t >> 6);
  int b = blockIdx.x;
  const int jrow = (b & 1) | ((b & 2) << 6) | ((b & 4) << 3) | ((b & 8) << 1) | ((b & 16) << 2);
  int oK = jrow * 32768 + w * 4096 + ((lane >> 2) & 3) * 256 + ((lane >> 4) * 4 + (lane & 3)) * 16;
  int tile = (b & 1) ? ((b >> 1) < 32 ? (b >> 1) : (b >> 1) + 16) : (b >= 224 ? 32 + ((b - 224) >> 1) : 255);
  int rs = tile < NMOD ? 6144 : 8192;
  int oT = (64 * w + (lane >> 3)) * rs + (tile < NMOD ? tile : tile - NMOD) * 128 + (lane & 7) * 16;
  unsigned head = (unsigned)(b >> 5) << 24;
  PIN(oK); PIN(oT);
  asm volatile("" : "+s"(rs), "+s"(head), "+s"(tile));
  SB();
  const __amdgpu_buffer_rsrc_t rK = rsrc((const char*)pK + head, 65536 * 256);
  v4u kv[128], tl[8];
  if (tile >= NSPEC) {
#pragma unroll
    for (int i = 0; i < LEAD + 2; ++i) kv[i] = ldnt(rK, oK + (i & 3) * 1024, urow(i >> 2) * 32768);
  } else {
    const __amdgpu_buffer_rsrc_t rT = rsrc(tile < NMOD ? pm : pq, 512 * rs);
#pragma unroll
    for (int i = 0; i < 8; ++i) tl[i] = ldnt(rT, oT, 8 * i * rs);
  }
  SB();
  asm volatile("" : "+s"(b), "+s"(tile));
  const int G = b >> 5, j = b & 31;
  unsigned char* ws = a.ws;
  const f32x4 Z4 = {0.f, 0.f, 0.f, 0.f};
  unsigned tb = t_start + ((w + (b >> 3)) & 7) * STAGGER;
  const bool is_mod = tile < NMOD, is_spec = tile < NSPEC, owner = is_spec && !is_mod;
  const bool is_ffn = tile == 255, is_wo = (tile >= 16 && tile < NMOD) || (tile >= NSPEC && !is_ffn);
  const int ow = tile - NMOD;
  const int os = tile < NMOD ? tile - 16 : tile - NSPEC + 32;
  const int fs = ((b >> 1) & 3) * 28 + (b >> 3);
  const int sh = (t & 1) * 16;
  const __amdgpu_buffer_rsrc_t rV = rsrc(a.V + (size_t)G * 65536 * 128, 65536 * 256);
  const int oV = jrow * 32768 + w * 4096 + lane * 16;
#define ISSUE(lo, hi, gap) do { _Pragma("unroll") for (int i_ = (lo); i_ < (hi); ++i_) { \
    if (i_ % 2 == 0 && i_ + (gap) > LEAD) { \
      const unsigned due_ = tb + (unsigned)(((i_ + (gap) - LEAD) * DT) >> 8); \
      while ((int)(now() - due_) < 0) __builtin_amdgcn_s_sleep(2); \
      SB(); \
    } \
    kv[i_] = ldnt(i_ < 64 ? rK : rV, (i_ < 64 ? oK : oV) + (i_ & 3) * 1024, urow((i_ & 63) >> 2) * 32768); \
    SB(); } } while (0)

  unsigned tag;
  const unsigned char* qg = ws + WS_Q + (G * 256 + (t >> 1)) * 8;
  const unsigned char* tp = ws + WS_T0 + G * 64 + (lane & 7) * 8;
  u64 g_q, g_t;
  if (is_spec) {
    const unsigned cv = ((const GAS u16*)(is_mod ? a.c : a.x))[t];
    const unsigned E = __hip_atomic_load((gu32*)(ws + WS_EPOCH) + (lane & 31), RLX_AGENT);
    const v4u x8 = *(const GAS v4u*)(a.x + lane * 8);
    SB();
    ISSUE(0, N_TILE, GAP1);
    if (!is_mod) ISSUE(N_TILE, N_MODPOLL, GAP1);
    unsigned* wimg = lds + L_W + w * 2048;
    stage_tile(wimg, tl, lane);
    CFENCE();
    v4u fr[4][2];
#pragma unroll
    for (int c = 0; c < 4; ++c) { fr[c][0] = wfrag(wimg + c * 512, lane, 0); fr[c][1] = wfrag(wimg + c * 512, lane, 1); }
    tag = make_tag(E, lane);
    if (b < 8 && t < 8) stg(ws + WS_T0 + t * 64 + b * 8, tag, t_start);
    float v = blo(cv);
    if (is_mod) {
      v = v / (1.f + expf(-v));
    } else {
      float s = 0.f;
#pragma unroll
      for (int k = 0; k < 4; ++k) s += blo(x8[k]) + bhi(x8[k]);
      const float mean = wave_sum(s) * (1.f / 512.f);
      float q = 0.f;
#pragma unroll
      for (int k = 0; k < 4; ++k) { const float d0 = blo(x8[k]) - mean, d1 = bhi(x8[k]) - mean; q = fmaf(d0, d0, q); q = fmaf(d1, d1, q); }
      const float var = wave_sum(q) * (1.f / 512.f);
      const float ln = rbf(rbf(v - rbf(mean)) / rbf(sqrtf(rbf(rbf(var) + 1e-5f))));
      SB();
      const unsigned char* mg = ws + WS_MOD + (t >> 1) * 8;
      u64 g_sa = ldg(mg), g_ca = ldg(mg + 256 * 8);
      SB();
      ISSUE(N_MODPOLL, N_MODCHECK, GAP1);
      if (!__all(tg(g_sa, tag) & tg(g_ca, tag))) {
        unsigned n = 0;
        do { g_sa = ldg(mg); g_ca = ldg(mg + 256 * 8); } while (!__all(tg(g_sa, tag) & tg(g_ca, tag)) && ++n < SPIN_CAP);
      }
      v = rbf(ln * rbf(1.f + blo((unsigned)g_ca >> sh))) + blo((unsigned)g_sa >> sh);
    }
    v4u b0, b1;
    vfrag(v, b0, b1);
#pragma unroll
    for (int c = 0; c < 4; ++c) {
      const f32x4 d = mfma(fr[c][1], b1, mfma(fr[c][0], b0, Z4));
      if ((lane & 15) == 0) *(f32x4*)(ldf + L_RED0 + w * 64 + c * 16 + (lane >> 2)) = d;
    }
    bar();
    if (w == 0) {
      const float* rp = ldf + L_RED0 + lane;
      const float s = ((rp[0] + rp[64]) + (rp[128] + rp[192])) + ((rp[256] + rp[320]) + (rp[384] + rp[448]));
      const unsigned p = pk(s, dpp<XOR1>(s));
      if (!(lane & 1)) stg(ws + (is_mod ? WS_MOD : WS_Q) + ((is_mod ? tile : ow) * 32 + (lane >> 1)) * 8, tag, p);
    }
    SB();
    if (is_mod) ISSUE(N_TILE, N_MODCHECK, GAP1);
    ISSUE(N_MODCHECK, N_QPOLL, GAP1);
    g_q = ldg(qg); g_t = ldg(tp);
    SB();
    ISSUE(N_QPOLL, N_QCHECK, GAP1);
  } else {
    const unsigned E = __hip_atomic_load((gu32*)(ws + WS_EPOCH) + (lane & 31), RLX_AGENT);
    SB();
    ISSUE(LEAD + 2, 12, GAP1);
    tag = make_tag(E, lane);
    if (b < 8 && t < 8) stg(ws + WS_T0 + t * 64 + b * 8, tag, t_start);
    SB();
    ISSUE(12, N_QPOLL, GAP1);
    g_q = ldg(qg); g_t = ldg(tp);
    SB();
    ISSUE(N_QPOLL, N_QCHECK, GAP1);
  }
  if (!__all(tg(g_q, tag))) {
    unsigned n = 0;
    do { g_q = ldg(qg); } while (!__all(tg(g_q, tag)) && ++n < SPIN_CAP);
  }
  {
    unsigned d = t_start - (unsigned)g_t;
    d = tg(g_t, tag) && d < 1000u ? d : 0u;
    d = max(d, dppu<XOR1>(d)); d = max(d, dppu<XOR2>(d)); d = max(d, dppu<XOR7>(d));
    tb -= __builtin_amdgcn_readfirstlane(d);
  }
  if (!(t & 1)) lds[L_Q + (t >> 1)] = (unsigned)g_q;
  bar();
  const v4u qf = *(const v4u*)(lds + L_Q + (lane & 3) * 64 + ((lane >> 4) * 4 + ((lane >> 2) & 3)) * 4);

  float sc[16];
  const bool b2 = lane & 4, b3 = lane & 8;
  v4u ax[8];
  u64 g_s, g_c;
  unsigned xo;
  const unsigned char* m2 = ws + WS_MOD + (owner ? 512 + ow * 4 + ((lane & 7) >> 1) : 768 + (t >> 1)) * 8;
  const int m2d = (owner ? 768 : 256) * 8;
#pragma unroll
  for (int u = 0; u < 16; ++u) {
    float e[4];
#pragma unroll
    for (int k = 0; k < 4; ++k) {
      const f32x4 d = mfma(kv[4 * u + k], qf, Z4);
      const float lo = b2 ? d[1] : d[0], hi = b2 ? d[3] : d[2];
      e[k] = b3 ? hi : lo;
    }
    SB();
    if (u == U_WLOAD) {
      const int nrec = is_ffn ? 512 * 1792 * 2 : (is_wo ? 4096 * 512 * 2 : 0);
      const __amdgpu_buffer_rsrc_t rA = rsrc(is_ffn ? a.wg : a.wo, nrec), rB = rsrc(is_ffn ? a.wu : a.wo, nrec), rC = rsrc(is_ffn ? a.wd : a.wo, nrec);
      const __amdgpu_buffer_rsrc_t rD = rsrc(a.wo, is_ffn ? 0 : nrec);
      const int vw = (os * 64 + 8 * ((lane >> 3) & 3) + (lane >> 5)) * 1024 + w * 128 + (lane & 7) * 16;
      const int vA = is_ffn ? (64 * w + (lane >> 1)) * 3584 + fs * 32 + (lane & 1) * 16 : vw;
      const int vB = is_ffn ? (fs * 16 + 2 * w) * 1024 + lane * 16 : vw;
      ax[0] = ldsh(rA, vA, 0);                     ax[1] = ldsh(rA, vA, is_ffn ? 32 * 3584 : 2048);
      ax[2] = ldsh(rB, vA, is_ffn ? 0 : 4096);     ax[3] = ldsh(rB, vA, is_ffn ? 32 * 3584 : 6144);
      ax[4] = ldnt(rC, vB, is_ffn ? 0 : 32768);    ax[5] = ldnt(rC, vB, is_ffn ? 1024 : 32768 + 2048);
      ax[6] = ldnt(rD, vB, 32768 + 4096);          ax[7] = ldnt(rD, vB, 32768 + 6144);
      g_s = ldg(m2); g_c = ldg(m2 + m2d);
      xo = ((const GAS u16*)a.x)[(ow & 63) * 8 + (lane & 7)];
      SB();
    }
    ISSUE(khi(u - 1), khi(u), u >= U_WLOAD ? GAP2 : GAP1);
    const float f0 = (b3 ? e[2] : e[0]) + dpp<XOR8>(b3 ? e[0] : e[2]);
    const float f1 = (b3 ? e[3] : e[1]) + dpp<XOR8>(b3 ? e[1] : e[3]);
    const float snd = b2 ? f0 : f1;
    const float from_lo = dpp<ROR4>(snd), from_hi = dpp<SHL4>(snd);
    const float s = (b2 ? f1 : f0) + (b2 ? from_lo : from_hi);
    sc[u] = rbf(rbf(s) * 0.08838834764831845f);
    if (u == U_WSTAGE) {
      if (is_ffn) {
#pragma unroll
        for (int i = 0; i < 4; ++i) *(v4u*)(lds + L_W + w * 1024 + i * 256 + lane * 4) = ax[i];
        stage_pairs(lds + L_WD + w * 512 + lane * 8, ax[4], ax[5]);
      } else {
        stage_tile(lds + L_W + w * 2048, ax, lane);
      }
      if (!__all(tg(g_s, tag) & tg(g_c, tag))) {
        unsigned n = 0;
        do { g_s = ldg(m2); g_c = ldg(m2 + m2d); } while (!__all(tg(g_s, tag) & tg(g_c, tag)) && ++n < SPIN_CAP);
      }
      SB();
    }
  }
  float md_s = blo((unsigned)g_s >> sh), md_c = blo((unsigned)g_c >> sh), x0 = blo(xo);

  float M = sc[0];
#pragma unroll
  for (int u = 1; u < 16; ++u) M = fmaxf(M, sc[u]);
  M = fmaxf(M, dpp<ROR4>(M)); M = fmaxf(M, dpp<XOR8>(M)); M = x32_max(x16_max(M));
  if (lane < 4) ldf[L_M + w * 4 + lane] = M;
  bar();
#pragma unroll
  for (int k = 0; k < 8; ++k) M = fmaxf(M, ldf[L_M + k * 4 + (lane & 3)]);
  {
    float l = 0.f;
#pragma unroll
    for (int u = 0; u < 16; ++u) { sc[u] = ex2((sc[u] - M) * LOG2E); l += sc[u]; }
    l += dpp<ROR4>(l); l += dpp<XOR8>(l); l = x32_sum(x16_sum(l));
    if (lane < 4) ldf[L_L + w * 4 + lane] = l;
  }
  bar();
  if (w == 0) {
    float L = 0.f;
#pragma unroll
    for (int k = 0; k < 8; ++k) L += ldf[L_L + k * 4 + (lane & 3)];
    if (lane < 8) stg(ws + WS_ML + (((G * 4 + (lane & 3)) * 32 + j) * 2 + (lane >> 2)) * 8, tag, asu(lane & 4 ? L : M));
  }
  SB();

  f32x4 acc[8];
#pragma unroll
  for (int d = 0; d < 8; ++d) acc[d] = Z4;
  const unsigned char* mlp = ws + WS_ML + ((G * 4 + (w & 3)) * 64 + lane) * 8;
  u64 gm;
  v4u wf[8];
  float z2 = 0.f, z3 = 0.f;
  const unsigned char* x2rep = ws + WS_X2 + ((b >> 1) & (XREP - 1)) * 2048;
  unsigned char* pa_st = ws + WS_PA + ((size_t)((G * 8 + w) * 32 + j) * 64 + lane) * 8;
  const unsigned char* n1_ld = owner ? ws + WS_PB + ((size_t)ow * 512 + t) * 8 : is_ffn ? x2rep + (t >> 1) * 8 : ws + WS_PA + ((size_t)(os & 63) * 2048 + w * 256 + lane) * 8;
  const unsigned char* n2_ld = owner ? ws + WS_PD + ((size_t)ow * (NFFN * 8) + w * 128 + lane) * 8 : x2rep;
  unsigned char* n_st = owner ? ws + WS_X2 + (((lane >> 3) * 2 + (lane & 1)) * 256 + ow * 4 + ((lane & 7) >> 1)) * 8
                      : is_ffn ? ws + WS_PD + ((size_t)((t >> 3) * NFFN + fs) * 8 + (t & 7)) * 8
                               : ws + WS_PB + ((size_t)((t >> 3) * 64 + (os & 63)) * 8 + (t & 7)) * 8;
  gu32* o_st = (gu32*)a.out + (ow & 63) * 4 + ((lane & 7) >> 1);
  LAS float* acc_st = (LAS float*)ldf + L_ACC + w * ACC_W + (lane & 3) * ACC_H + (lane >> 4) * 32;
  LAS float* acc_ld = (LAS float*)ldf + L_ACC + (t >> 7) * ACC_H + (t & 127);
  LAS float* f_ld = (LAS float*)ldf + L_F + (t >> 7);
  LAS float* o_ld = (LAS float*)ldf + L_O + lane * 8;
  LAS float* o_sl = o_ld + w;
  LAS float* r2_ld = (LAS float*)ldf + L_RED2 + (lane & 31) * 8;
  LAS float* r2_st = r2_ld + w;
  LAS float* r3_ld = (LAS float*)ldf + (lane & 7) * 8;
  LAS float* r3_st = r3_ld + w;
  LAS float* ln_st = (LAS float*)ldf + L_LN + w * 2;
  unsigned tagv = tag;
#pragma unroll
  for (int k = 0; k < 8; ++k) {
    v4u pf;
    pf[0] = pk(sc[2 * k], dpp<SHL4>(sc[2 * k])); pf[1] = pk(dpp<SHL8>(sc[2 * k]), dpp<SHL12>(sc[2 * k]));
    pf[2] = pk(sc[2 * k + 1], dpp<SHL4>(sc[2 * k + 1])); pf[3] = pk(dpp<SHL8>(sc[2 * k + 1]), dpp<SHL12>(sc[2 * k + 1]));
    SB();
    if (k == K_MLUSE && w < 4) {
      if (!__all(tg(gm, tag))) {
        unsigned n = 0;
        do { gm = ldg(mlp); } while (!__all(tg(gm, tag)) && ++n < SPIN_CAP);
      }
      const float v = asf((unsigned)gm);
      const float Mg = wave_max((lane & 1) ? -INFINITY : v);
      const float f = ex2((v - Mg) * LOG2E), fl = f * dpp<XOR1>(v);
      const float Lg = wave_sum((lane & 1) ? 0.f : fl);
      if (lane == 2 * j) ldf[L_F + w] = f / Lg;
      SB();
    }
    if (k == 7) {
      const unsigned* wimg = lds + L_W + w * (is_ffn ? 1024 : 2048);
#pragma unroll
      for (int i = 0; i < 8; ++i) wf[i] = wfrag(wimg + (i >> 1) * 512, lane, i & 1);
      if (is_ffn) {
#pragma unroll
        for (int i = 0; i < 8; ++i) wf[4 + (i >> 2)][i & 3] = lds[L_WD + i * 512 + t];
      }
#pragma unroll
      for (int i = 0; i < 8; ++i) PIN(wf[i]);
      PIN(pa_st); PIN(n1_ld); PIN(n2_ld); PIN(n_st); PIN(o_st); PIN(acc_st); PIN(acc_ld); PIN(f_ld); PIN(o_ld); PIN(o_sl); PIN(r2_ld); PIN(r2_st);
      PIN(r3_ld); PIN(r3_st); PIN(ln_st); PIN(z2); PIN(z3); PIN(md_s); PIN(md_c); PIN(x0); PIN(tagv);
      SB();
    }
    v4u ve[4], vo[4];
#pragma unroll
    for (int p = 0; p < 4; ++p) {
#pragma unroll
      for (int dq = 0; dq < 4; ++dq) {
        const unsigned r0 = kv[64 + 8 * k + 2 * p][dq], r1 = kv[64 + 8 * k + 2 * p + 1][dq];
        ve[dq][p] = __builtin_amdgcn_perm(r1, r0, 0x05040100u);
        vo[dq][p] = __builtin_amdgcn_perm(r1, r0, 0x07060302u);
      }
      if (k == 7) SB();
    }
#pragma unroll
    for (int dq = 0; dq < 4; ++dq) {
      acc[2 * dq] = mfma(ve[dq], pf, acc[2 * dq]);
      acc[2 * dq + 1] = mfma(vo[dq], pf, acc[2 * dq + 1]);
    }
    SB();
    if (k == K_MLLOAD) { gm = ldg(mlp); SB(); }
    ISSUE(64 + 8 * k + WV, 64 + 8 * k + WV + 8 < 128 ? 64 + 8 * k + WV + 8 : 128, GAP2);
  }
#undef ISSUE
  if ((lane & 15) < 4) {
#pragma unroll
    for (int d = 0; d < 8; ++d) *(LAS f32x4*)(acc_st + d * 4) = acc[d];
  }
  bar();
  {
    const float f = *f_ld;
    const float o = ((acc_ld[0] + acc_ld[ACC_W]) + (acc_ld[2 * ACC_W] + acc_ld[3 * ACC_W])) + ((acc_ld[4 * ACC_W] + acc_ld[5 * ACC_W]) + (acc_ld[6 * ACC_W] + acc_ld[7 * ACC_W]));
    stg(pa_st, tagv, asu(o * f));
  }
  if (is_wo) {
    *o_sl = poll_sum4(n1_ld, tagv);
    bar();
    v4u o0, o1;
    vfrag(sum8(o_ld), o0, o1);
    const f32x4 d0 = mfma(o1, wf[1], mfma(o0, wf[0], Z4)), d1 = mfma(o1, wf[3], mfma(o0, wf[2], Z4));
    const f32x4 d2 = mfma(o1, wf[5], mfma(o0, wf[4], Z4)), d3 = mfma(o1, wf[7], mfma(o0, wf[6], Z4));
    const float lo = (lane & 16) ? d1[0] : d0[0], hi = (lane & 16) ? d3[0] : d2[0];
    stg(n_st, tagv, asu((lane & 32) ? hi : lo));
  } else if (owner) {
    float y = 0.f;
    r3_st[L_RED3] = red8(poll_one(n1_ld, tagv));
    bar();
    if (w == 0) {
      y = rbf(x0 + rbf(md_s * rbf(sum8(r3_ld + L_RED3))));
      const unsigned yo = dppu<XOR1>(asu(y));
      stg(n_st, tagv, __builtin_amdgcn_perm(asu(y), yo, (lane & 1) ? 0x07060302u : 0x03020706u));
    }
    float s = 0.f;
    if (w < 7) s = red8(poll_sum2(n2_ld, tagv));
    r3_st[L_RED4] = s;
    bar();
    if (w == 0) {
      const float o = y + rbf(md_c * rbf(sum8(r3_ld + L_RED4)));
      const unsigned p = pk(o, dpp<XOR1>(o));
      if (lane < 8 && !(lane & 1)) __hip_atomic_store(o_st, p, RLX_AGENT);
      if (ow == 0 && lane < 31) __hip_atomic_store((gu32*)(ws + WS_EPOCH) + lane, tagv, RLX_AGENT);
    }
  } else if (is_ffn) {
    float cb1 = rbf(1.f + md_c);
    PIN(cb1);
    unsigned gp;
    const unsigned gx = poll_two(n1_ld, n2_ld, tagv, gp);
    const float x2 = blo(gx >> sh), piv = blo(gp), dv = x2 - piv;
    *(LAS f32x2*)ln_st = f32x2{wave_sum(dv), wave_sum(dv * dv)};
    bar();
    const f32x4 qq = (*(const f32x4*)(ldf + L_LN) + *(const f32x4*)(ldf + L_LN + 4)) + (*(const f32x4*)(ldf + L_LN + 8) + *(const f32x4*)(ldf + L_LN + 12));
    const float md = (qq[0] + qq[2]) * (1.f / 512.f);
    const float var = fmaxf(fmaf(-md, md, (qq[1] + qq[3]) * (1.f / 512.f)), 0.f);
    const float ri = rcp(rbf(__builtin_amdgcn_sqrtf(rbf(rbf(var) + 1e-5f))));
    v4u b0, b1;
    vfrag(rbf(rbf(rbf(x2 - rbf(piv + md)) * ri) * cb1) + md_s, b0, b1);
    const f32x4 d0 = mfma(b1, wf[1], mfma(b0, wf[0], Z4)), d1 = mfma(b1, wf[3], mfma(b0, wf[2], Z4));
    *r2_st = (lane & 16) ? d1[0] : d0[0];
    bar();
    const float s = rbf(sum8(r2_ld));
    const auto gu = __builtin_amdgcn_permlane16_swap(asu(s), asu(s), false, false);
    const float gt = asf(gu[0]), up = asf(gu[1]);
    const float z = (-2.f * 0.7978845608028654f * LOG2E) * gt * fmaf(0.044715f * gt, gt, 1.f);
    const float act = rbf(gt * rcp(1.f + ex2(z))) * up;
    stg(n_st, tagv, asu(dot16(pk(act, dpp<XOR1>(act)), wf[4], wf[5], z2, z3)));
  }
}

extern "C" void kernel_launch(void* const* d_in, const int* in_sizes, int n_in, void* d_out, int out_size, void* d_ws, size_t ws_size, hipStream_t stream) {
  Args a;
  a.x = (const u16*)d_in[0]; a.K = (const u16*)d_in[1]; a.V = (const u16*)d_in[2]; a.wq = (const u16*)d_in[3]; a.wo = (const u16*)d_in[4];
  a.wg = (const u16*)d_in[6]; a.wu = (const u16*)d_in[7]; a.wd = (const u16*)d_in[8]; a.c = (const u16*)d_in[9]; a.wmod = (const u16*)d_in[10];
  a.out = (u16*)d_out; a.ws = (unsigned char*)d_ws;
  hipLaunchKernelGGL(fused_block, dim3(NWG), dim3(NTHR), 0, stream, a);
}
```
